```python
import torch

def _softmax(x, axis=-1):
    return torch.softmax(x, dim=axis)
def _gelu(x):
    return torch.nn.functional.gelu(x, approximate="tanh")
def _silu(x):
    return torch.nn.functional.silu(x)
def _sigmoid(x):
    return torch.sigmoid(x)
def _layernorm(x, axis=-1, eps=1e-5):
    m = x.mean(dim=axis, keepdim=True)
    v = x.var(dim=axis, keepdim=True, unbiased=False)
    return (x - m) / torch.sqrt(v + eps)
def _scatter_add(tgt, idx, src, axis=0):
    return tgt.clone().index_add_(axis, idx, src)
def _segment_sum(src, seg, n):
    out = torch.zeros((n,) + src.shape[1:], dtype=src.dtype)
    return out.index_add_(0, seg, src)
def _pad(x, pw):
    flat = tuple(v for pair in reversed(pw) for v in pair)
    return torch.nn.functional.pad(x, flat)

kernel_name = 'standalone__groupnorm__prompt'

def setup_inputs():

    _w = (lambda a: (lambda t: t.to(torch.bfloat16) if t.is_floating_point() else t)(torch.as_tensor(a)))
    B = 1
    G = 32
    cpg = max(1, 448 // G)
    C = G * cpg
    H, W = 16, 4096
    return {
        "x": _w(abs((lambda *s: torch.randn(*s, dtype=torch.bfloat16) * 0.05)(B, C, H, W))),
        "g": _w((lambda *s: torch.randn(*s, dtype=torch.bfloat16) * 0.05)(C) + 1.0),
        "b": _w((lambda *s: torch.randn(*s, dtype=torch.bfloat16) * 0.4)(C)),
    }

def reference(x, g, b):
    G = 32
    B, C, H, W = x.shape[0], x.shape[1], x.shape[2], x.shape[3]
    xr = x.reshape(B, G, C // G, H, W)
    mu = xr.mean(axis=(2, 3, 4), keepdims=True)
    var = ((xr - mu) ** 2).mean(axis=(2, 3, 4), keepdims=True)
    xn = (xr - mu) / torch.sqrt(var + 1e-5)
    xn = xn.reshape(B, C, H, W)
    return xn * g[None, :, None, None] + b[None, :, None, None]
```

```cpp
#include <hip/hip_runtime.h>
#include <hip/hip_bf16.h>
#include <stdint.h>

typedef unsigned v4u __attribute__((ext_vector_type(4)));
typedef float v2f __attribute__((ext_vector_type(2)));

constexpr int G = 32, CPG = 14, HW = 65536;
constexpr int SPLIT = 16;

__device__ __forceinline__ float lo(unsigned u) { return __uint_as_float(u << 16); }
__device__ __forceinline__ float hi(unsigned u) { return __uint_as_float(u & 0xffff0000u); }

__global__ void __launch_bounds__(256) stats_k(const v4u* __restrict__ x, float2* __restrict__ part) {
    const int b = blockIdx.x;
    const v4u* p = x + (size_t)b * 512;
    v4u a0 = p[threadIdx.x], a1 = p[threadIdx.x + 256];
    float s = 0.f;
    #pragma unroll
    for (int j = 0; j < 4; ++j) s += lo(a0[j]) + hi(a0[j]) + lo(a1[j]) + hi(a1[j]);
    __shared__ float red[8];
    for (int m = 32; m >= 1; m >>= 1) s += __shfl_xor(s, m);
    if ((threadIdx.x & 63) == 0) red[threadIdx.x >> 6] = s;
    __syncthreads();
    float mean = (red[0] + red[1] + red[2] + red[3]) * (1.f / 4096.f);
    float q = 0.f;
    #pragma unroll
    for (int j = 0; j < 4; ++j) {
        float d;
        d = lo(a0[j]) - mean; q += d * d; d = hi(a0[j]) - mean; q += d * d;
        d = lo(a1[j]) - mean; q += d * d; d = hi(a1[j]) - mean; q += d * d;
    }
    for (int m = 32; m >= 1; m >>= 1) q += __shfl_xor(q, m);
    if ((threadIdx.x & 63) == 0) red[4 + (threadIdx.x >> 6)] = q;
    __syncthreads();
    if (threadIdx.x == 0) part[b] = make_float2(mean, red[4] + red[5] + red[6] + red[7]);
}

__global__ void __launch_bounds__(256) norm_k(const v4u* __restrict__ x, const float2* __restrict__ part,
                                              const __hip_bfloat16* __restrict__ gam, const __hip_bfloat16* __restrict__ bet,
                                              v4u* __restrict__ y) {
    const int b = blockIdx.x;
    const int c = b / SPLIT, g = c / CPG;
    const v4u* p = x + (size_t)b * 512;
    v4u a0 = p[threadIdx.x], a1 = p[threadIdx.x + 256];
    constexpr int NP = CPG * SPLIT;
    float2 pv = make_float2(0.f, 0.f);
    const int lane = threadIdx.x & 63;
    float m4[4], q4[4];
    float s = 0.f;
    #pragma unroll
    for (int k = 0; k < 4; ++k) {
        int i = lane + 64 * k;
        float2 t = (i < NP) ? part[g * NP + i] : make_float2(0.f, 0.f);
        m4[k] = t.x; q4[k] = t.y; s += t.x;
    }
    for (int m = 32; m >= 1; m >>= 1) s += __shfl_xor(s, m);
    float mean = s * (1.f / NP);
    float q = 0.f;
    #pragma unroll
    for (int k = 0; k < 4; ++k) {
        int i = lane + 64 * k;
        float d = m4[k] - mean;
        q += (i < NP) ? (q4[k] + 4096.f * d * d) : 0.f;
    }
    for (int m = 32; m >= 1; m >>= 1) q += __shfl_xor(q, m);
    float var = q * (1.f / (float)(CPG * HW));
    float rstd = 1.0f / sqrtf(var + 1e-5f);
    float sc = rstd * __bfloat162float(gam[c]);
    float sh = __bfloat162float(bet[c]) - mean * sc;
    v4u o0, o1;
    #pragma unroll
    for (int j = 0; j < 4; ++j) {
        __hip_bfloat162 r0 = __float22bfloat162_rn(make_float2(lo(a0[j]) * sc + sh, hi(a0[j]) * sc + sh));
        __hip_bfloat162 r1 = __float22bfloat162_rn(make_float2(lo(a1[j]) * sc + sh, hi(a1[j]) * sc + sh));
        o0[j] = *(unsigned*)&r0; o1[j] = *(unsigned*)&r1;
    }
    v4u* q0 = y + (size_t)b * 512;
    q0[threadIdx.x] = o0; q0[threadIdx.x + 256] = o1;
}

extern "C" void kernel_launch(void* const* d_in, const int* in_sizes, int n_in,
                              void* d_out, int out_size, void* d_ws, size_t ws_size,
                              hipStream_t stream) {
    const v4u* x = (const v4u*)d_in[0];
    const __hip_bfloat16* g = (const __hip_bfloat16*)d_in[1];
    const __hip_bfloat16* b = (const __hip_bfloat16*)d_in[2];
    float2* part = (float2*)d_ws;
    const int nb = G * CPG * SPLIT;
    stats_k<<<nb, 256, 0, stream>>>(x, part);
    norm_k<<<nb, 256, 0, stream>>>(x, part, g, b, (v4u*)d_out);
}
```

```cpp
#include <hip/hip_runtime.h>
#include <hip/hip_bf16.h>
#include <stdint.h>

typedef unsigned v4u __attribute__((ext_vector_type(4)));
typedef float v2f __attribute__((ext_vector_type(2)));
typedef __bf16 v2bf __attribute__((ext_vector_type(2)));
#define GAS __attribute__((address_space(1)))
typedef GAS unsigned long long gu64;
#define RLX_AGENT __ATOMIC_RELAXED, __HIP_MEMORY_SCOPE_AGENT

constexpr int G = 32, CPG = 14, HW = 65536;
constexpr int WPG = 8;
constexpr int NT = 1024;
constexpr int SEG = HW / WPG;

__device__ __forceinline__ v2f unpack(unsigned u) { return (v2f){__uint_as_float(u << 16), __uint_as_float(u & 0xffff0000u)}; }
__device__ __forceinline__ unsigned pack(v2f f) { v2bf r = __builtin_convertvector(f, v2bf); return __builtin_bit_cast(unsigned, r); }
__device__ __forceinline__ float wsum(float s) {
    #pragma unroll
    for (int m = 32; m >= 1; m >>= 1) s += __shfl_xor(s, m);
    return s;
}

#define LD_AUX 2
#define ST_AUX 16

__global__ void __launch_bounds__(NT, 4) gn_fused(const void* __restrict__ x, const unsigned short* __restrict__ gam,
                                                  const unsigned short* __restrict__ bet, void* __restrict__ y,
                                                  unsigned long long* __restrict__ slots) {
    const int b = blockIdx.x;
    const int g = b >> 3, j = b & 7;
    const int t = threadIdx.x, lane = t & 63, wave = t >> 6;
    const unsigned base = (unsigned)(g * CPG * HW + j * SEG) * 2u;
    const __amdgpu_buffer_rsrc_t rx = __builtin_amdgcn_make_buffer_rsrc((char*)x + base, 0, G * CPG * HW * 2 - base, 0x00020000);
    const __amdgpu_buffer_rsrc_t ry = __builtin_amdgcn_make_buffer_rsrc((char*)y + base, 0, G * CPG * HW * 2 - base, 0x00020000);
    v4u a[CPG];
    #pragma unroll
    for (int k = 0; k < CPG; ++k)
        a[k] = __builtin_amdgcn_raw_buffer_load_b128(rx, t * 16, k * HW * 2, LD_AUX);
    __builtin_amdgcn_sched_barrier(0);
    const int ck = g * CPG + (lane < CPG ? lane : 0);
    const float gl = __uint_as_float((unsigned)gam[ck] << 16), bl = __uint_as_float((unsigned)bet[ck] << 16);
    __builtin_amdgcn_sched_barrier(0);

    __shared__ float red[2][16];
    __shared__ float stat[2];
    v2f s2 = {0.f, 0.f};
    #pragma unroll
    for (int k = 0; k < CPG; ++k) {
        s2 += (unpack(a[k][0]) + unpack(a[k][1])) + (unpack(a[k][2]) + unpack(a[k][3]));
    }
    #pragma unroll
    for (int k = 0; k < CPG; ++k) asm volatile("" : "+v"(a[k]));
    float s = wsum(s2.x + s2.y);
    const float wm = s * (1.f / (64 * 8 * CPG));
    v2f q2v = {0.f, 0.f}, q2w = {0.f, 0.f};
    const v2f wm2 = {wm, wm};
    #pragma unroll
    for (int k = 0; k < CPG; ++k) {
        #pragma unroll
        for (int i = 0; i < 4; i += 2) {
            v2f d0 = unpack(a[k][i]) - wm2, d1 = unpack(a[k][i + 1]) - wm2;
            q2v = __builtin_elementwise_fma(d0, d0, q2v); q2w = __builtin_elementwise_fma(d1, d1, q2w);
        }
    }
    #pragma unroll
    for (int k = 0; k < CPG; ++k) asm volatile("" : "+v"(a[k]));
    float q = wsum((q2v.x + q2v.y) + (q2w.x + q2w.y));
    if (lane == 0) { red[0][wave] = wm; red[1][wave] = q; }
    __syncthreads();
    if (wave == 0) {
        float m = red[0][lane & 15], qq = red[1][lane & 15];
        float ms = m;
        #pragma unroll
        for (int o = 8; o >= 1; o >>= 1) ms += __shfl_xor(ms, o);
        const float bm = ms * (1.f / 16.f);
        float d = m - bm;
        float q2 = qq + (float)(64 * 8 * CPG) * d * d;
        #pragma unroll
        for (int o = 8; o >= 1; o >>= 1) q2 += __shfl_xor(q2, o);
        gu64* sl = (gu64*)slots + g * 32;
        if (lane < 2) {
            unsigned v = __float_as_uint(lane == 0 ? bm : q2);
            __hip_atomic_store(sl + j * 2 + lane, (1ull << 32) | v, RLX_AGENT);
        }
        unsigned long long r;
        for (unsigned spins = 0; spins < (1u << 20); ++spins) {
            r = __hip_atomic_load(sl + (lane & 15), RLX_AGENT);
            if (__all((unsigned)(r >> 32) == 1u)) break;
            __builtin_amdgcn_s_sleep(1);
        }
        float v = __uint_as_float((unsigned)r);
        float vm = __shfl(v, (lane & 7) * 2), vq = __shfl(v, (lane & 7) * 2 + 1);
        float gs = vm;
        #pragma unroll
        for (int o = 4; o >= 1; o >>= 1) gs += __shfl_xor(gs, o);
        const float mean = gs * (1.f / 8.f);
        float dd = vm - mean;
        float gq = vq + (float)(SEG * CPG) * dd * dd;
        #pragma unroll
        for (int o = 4; o >= 1; o >>= 1) gq += __shfl_xor(gq, o);
        const float var = gq * (1.f / (float)(CPG * HW));
        if (lane == 0) { stat[0] = mean; stat[1] = 1.0f / sqrtf(var + 1e-5f); }
    }
    __syncthreads();
    const float mean = stat[0], rstd = stat[1];
    const float scl = rstd * gl, shl = bl - mean * scl;
    #pragma unroll
    for (int k = 0; k < CPG; ++k) {
        const float sc = __uint_as_float(__builtin_amdgcn_readlane(__float_as_uint(scl), k)), sh = __uint_as_float(__builtin_amdgcn_readlane(__float_as_uint(shl), k));
        const v2f sc2 = {sc, sc}, sh2 = {sh, sh};
        v4u o;
        #pragma unroll
        for (int i = 0; i < 4; ++i) o[i] = pack(__builtin_elementwise_fma(unpack(a[k][i]), sc2, sh2));
        __builtin_amdgcn_raw_buffer_store_b128(o, ry, t * 16 + k * HW * 2, 0, ST_AUX);
    }
}

extern "C" void kernel_launch(void* const* d_in, const int* in_sizes, int n_in,
                              void* d_out, int out_size, void* d_ws, size_t ws_size,
                              hipStream_t stream) {

    gn_fused<<<G * WPG, NT, 0, stream>>>(d_in[0], (const unsigned short*)d_in[1], (const unsigned short*)d_in[2],
                                          d_out, (unsigned long long*)d_ws);
}
```

```cpp
#include <hip/hip_runtime.h>
#include <hip/hip_bf16.h>
#include <stdint.h>

typedef unsigned v4u __attribute__((ext_vector_type(4)));
typedef float v2f __attribute__((ext_vector_type(2)));
typedef __bf16 v2bf __attribute__((ext_vector_type(2)));
#define GAS __attribute__((address_space(1)))
typedef GAS unsigned long long gu64;
#define RLX_AGENT __ATOMIC_RELAXED, __HIP_MEMORY_SCOPE_AGENT

constexpr int G = 32, CPG = 14, HW = 65536;
constexpr int WPG = 8;
constexpr int NT = 1024;
constexpr int SEG = HW / WPG;

__device__ __forceinline__ v2f unpack(unsigned u) { return (v2f){__uint_as_float(u << 16), __uint_as_float(u & 0xffff0000u)}; }
__device__ __forceinline__ unsigned pack(v2f f) { v2bf r = __builtin_convertvector(f, v2bf); return __builtin_bit_cast(unsigned, r); }
__device__ __forceinline__ float wsum(float s) {
    #pragma unroll
    for (int m = 32; m >= 1; m >>= 1) s += __shfl_xor(s, m);
    return s;
}
__device__ __forceinline__ unsigned or16(unsigned v) {
    #pragma unroll
    for (int m = 8; m >= 1; m >>= 1) v |= __shfl_xor(v, m);
    return v;
}

#define LD_AUX 2
#define ST_AUX 16
#define MAPPING 1
#define TLBWARM 0

__global__ void __launch_bounds__(NT, 4) gn_fused(const void* __restrict__ x, const unsigned short* __restrict__ gam,
                                                  const unsigned short* __restrict__ bet, void* __restrict__ y,
                                                  unsigned long long* __restrict__ boards) {
    const int b = blockIdx.x;
    const int g = (b & 7) * 4 + (b >> 6), j = (b >> 3) & 7;
    const int t = threadIdx.x, lane = t & 63;
    const int wave = __builtin_amdgcn_readfirstlane(t >> 6);
    gu64* board = (gu64*)boards + g * 32 + (lane & 15);
    unsigned long long entry = 0;
    if (wave == 0) entry = __hip_atomic_load(board, RLX_AGENT);
    __builtin_amdgcn_sched_barrier(0);

    const unsigned base = (unsigned)(g * CPG * HW + j * SEG) * 2u;
    const __amdgpu_buffer_rsrc_t rx = __builtin_amdgcn_make_buffer_rsrc((char*)x + base, 0, G * CPG * HW * 2 - base, 0x00020000);
    const __amdgpu_buffer_rsrc_t ry = __builtin_amdgcn_make_buffer_rsrc((char*)y + base, 0, G * CPG * HW * 2 - base, 0x00020000);
    v4u a[CPG];
    #pragma unroll
    for (int k = 0; k < CPG; ++k) {
        a[k] = __builtin_amdgcn_raw_buffer_load_b128(rx, t * 16, k * HW * 2, LD_AUX);
        __builtin_amdgcn_sched_barrier(0);
    }
    const int ck = g * CPG + (lane < CPG ? lane : 0);
    const float gl = __uint_as_float((unsigned)gam[ck] << 16), bl = __uint_as_float((unsigned)bet[ck] << 16);
    __builtin_amdgcn_sched_barrier(0);

    const int sh_j = 4 * j, snd = (lane & 15) >> 1;
    unsigned nonce = 0;
    if (wave == 0) {
        const unsigned tags = (unsigned)(entry >> 32);
        const unsigned seen = or16(snd == j ? 0u : 1u << ((tags >> sh_j) & 15u));
        nonce = __builtin_amdgcn_readfirstlane(__builtin_ctz(~seen));
        if (lane < 16 && snd == j) {
            const unsigned nt = (tags & ~(15u << sh_j)) | (nonce << sh_j);
            __hip_atomic_store(board, ((unsigned long long)nt << 32) | (unsigned)entry, RLX_AGENT);
        }
    }

    __shared__ float red[2][16];
    __shared__ float stat[2];
    v2f s2 = {0.f, 0.f};
    unsigned long long cur = 0;
    #pragma unroll
    for (int k = 0; k < CPG; ++k) {
        s2 += (unpack(a[k][0]) + unpack(a[k][1])) + (unpack(a[k][2]) + unpack(a[k][3]));
        asm volatile("" : "+v"(s2) :: "memory");
        if (k == 9 && wave == 0) cur = __hip_atomic_load(board, RLX_AGENT);
    }
    #pragma unroll
    for (int k = 0; k < CPG; ++k) asm volatile("" : "+v"(a[k]));
    float s = wsum(s2.x + s2.y);
    const float wm = s * (1.f / (64 * 8 * CPG));
    v2f q2v = {0.f, 0.f}, q2w = {0.f, 0.f};
    const v2f wm2 = {wm, wm};
    #pragma unroll
    for (int k = 0; k < CPG; ++k) {
        #pragma unroll
        for (int i = 0; i < 4; i += 2) {
            v2f d0 = unpack(a[k][i]) - wm2, d1 = unpack(a[k][i + 1]) - wm2;
            q2v = __builtin_elementwise_fma(d0, d0, q2v); q2w = __builtin_elementwise_fma(d1, d1, q2w);
        }
    }
    #pragma unroll
    for (int k = 0; k < CPG; ++k) asm volatile("" : "+v"(a[k]));
    float q = wsum((q2v.x + q2v.y) + (q2w.x + q2w.y));
    if (lane == 0) { red[0][wave] = wm; red[1][wave] = q; }
    __syncthreads();
    if (wave == 0) {
        float m = red[0][lane & 15], qq = red[1][lane & 15];
        float ms = m;
        #pragma unroll
        for (int o = 8; o >= 1; o >>= 1) ms += __shfl_xor(ms, o);
        const float bm = ms * (1.f / 16.f);
        float d = m - bm;
        float q2 = qq + (float)(64 * 8 * CPG) * d * d;
        #pragma unroll
        for (int o = 8; o >= 1; o >>= 1) q2 += __shfl_xor(q2, o);
        const unsigned mine = __float_as_uint((lane & 1) ? q2 : bm);
        unsigned sent = 0; bool first = true;
        for (unsigned spins = 0; spins < (1u << 22); ++spins) {
            const unsigned tags = (unsigned)(cur >> 32);
            const unsigned own = (snd == j) ? (nonce << sh_j) : (tags & (15u << (4 * snd)));
            const unsigned view = __builtin_amdgcn_readfirstlane(or16((lane & 1) ? 0u : own));
            if (first || view != sent) {
                if (lane < 16 && snd == j) __hip_atomic_store(board, ((unsigned long long)view << 32) | mine, RLX_AGENT);
                sent = view; first = false;
            }
            if (__all(snd == j || ((tags >> sh_j) & 15u) == nonce)) break;
            cur = __hip_atomic_load(board, RLX_AGENT);
        }
        float v = __uint_as_float(snd == j ? mine : (unsigned)cur);
        float vm = __shfl(v, (lane & 7) * 2), vq = __shfl(v, (lane & 7) * 2 + 1);
        float gs = vm;
        #pragma unroll
        for (int o = 4; o >= 1; o >>= 1) gs += __shfl_xor(gs, o);
        const float mean = gs * (1.f / 8.f);
        float dd = vm - mean;
        float gq = vq + (float)(SEG * CPG) * dd * dd;
        #pragma unroll
        for (int o = 4; o >= 1; o >>= 1) gq += __shfl_xor(gq, o);
        const float var = gq * (1.f / (float)(CPG * HW));
        if (lane == 0) { stat[0] = mean; stat[1] = 1.0f / sqrtf(var + 1e-5f); }
    }
    __syncthreads();
    const float mean = stat[0], rstd = stat[1];
    const float scl = rstd * gl, shl = bl - mean * scl;
    #pragma unroll
    for (int k = 0; k < CPG; ++k) {
        const float sc = __uint_as_float(__builtin_amdgcn_readlane(__float_as_uint(scl), k));
        const float sh = __uint_as_float(__builtin_amdgcn_readlane(__float_as_uint(shl), k));
        const v2f sc2 = {sc, sc}, sh2 = {sh, sh};
        v4u o;
        #pragma unroll
        for (int i = 0; i < 4; ++i) o[i] = pack(__builtin_elementwise_fma(unpack(a[k][i]), sc2, sh2));
        __builtin_amdgcn_raw_buffer_store_b128(o, ry, t * 16 + k * HW * 2, 0, ST_AUX);
    }
}

extern "C" void kernel_launch(void* const* d_in, const int* in_sizes, int n_in,
                              void* d_out, int out_size, void* d_ws, size_t ws_size,
                              hipStream_t stream) {
    gn_fused<<<G * WPG, NT, 0, stream>>>(d_in[0], (const unsigned short*)d_in[1], (const unsigned short*)d_in[2],
                                          d_out, (unsigned long long*)d_ws);
}
```

```cpp
#include <hip/hip_runtime.h>
#include <hip/hip_bf16.h>
#include <stdint.h>

typedef unsigned v4u __attribute__((ext_vector_type(4)));
typedef float v2f __attribute__((ext_vector_type(2)));
typedef __bf16 v2bf __attribute__((ext_vector_type(2)));
#define GAS __attribute__((address_space(1)))
typedef GAS unsigned long long gu64;
#define RLX_AGENT __ATOMIC_RELAXED, __HIP_MEMORY_SCOPE_AGENT

constexpr int G = 32, CPG = 14, HW = 65536;
constexpr int WPG = 8;
constexpr int NT = 1024;
constexpr int SEG = HW / WPG;

__device__ __forceinline__ v2f unpack(unsigned u) { return (v2f){__uint_as_float(u << 16), __uint_as_float(u & 0xffff0000u)}; }
__device__ __forceinline__ unsigned pack(v2f f) { v2bf r = __builtin_convertvector(f, v2bf); return __builtin_bit_cast(unsigned, r); }
__device__ __forceinline__ float wsum(float s) {
    #pragma unroll
    for (int m = 32; m >= 1; m >>= 1) s += __shfl_xor(s, m);
    return s;
}
__device__ __forceinline__ unsigned or16(unsigned v) {
    #pragma unroll
    for (int m = 8; m >= 1; m >>= 1) v |= __shfl_xor(v, m);
    return v;
}

#define STAMP(v) unsigned long long v = __builtin_amdgcn_s_memrealtime(); asm volatile("s_waitcnt lgkmcnt(0)" : "+s"(v) :: "memory")
#define KAMP 4
#define TA t0
#define TB t0
#define LD_AUX 2
#define ST_AUX 16
#define MAPPING 0
#define TLBWARM 0

__global__ void __launch_bounds__(NT, 4) gn_fused(const void* __restrict__ x, const unsigned short* __restrict__ gam,
                                                  const unsigned short* __restrict__ bet, void* __restrict__ y,
                                                  unsigned long long* __restrict__ boards) {
    STAMP(t0);
    const int b = blockIdx.x;
    const int g = b >> 3, j = b & 7;
    const int t = threadIdx.x, lane = t & 63;
    const int wave = __builtin_amdgcn_readfirstlane(t >> 6);
    gu64* board = (gu64*)boards + g * 32 + (lane & 15);
    unsigned long long entry = 0;
    if (wave == 0) entry = __hip_atomic_load(board, RLX_AGENT);
    __builtin_amdgcn_sched_barrier(0);

    const unsigned base = (unsigned)(g * CPG * HW + j * SEG) * 2u;
    const __amdgpu_buffer_rsrc_t rx = __builtin_amdgcn_make_buffer_rsrc((char*)x + base, 0, G * CPG * HW * 2 - base, 0x00020000);
    const __amdgpu_buffer_rsrc_t ry = __builtin_amdgcn_make_buffer_rsrc((char*)y + base, 0, G * CPG * HW * 2 - base, 0x00020000);
    v4u a[CPG];
    #pragma unroll
    for (int k = 0; k < CPG; ++k) {
        a[k] = __builtin_amdgcn_raw_buffer_load_b128(rx, t * 16, k * HW * 2, LD_AUX);
        __builtin_amdgcn_sched_barrier(0);
    }
    const int ck = g * CPG + (lane < CPG ? lane : 0);
    const float gl = __uint_as_float((unsigned)gam[ck] << 16), bl = __uint_as_float((unsigned)bet[ck] << 16);
    __builtin_amdgcn_sched_barrier(0);

    const int sh_j = 4 * j, snd = (lane & 15) >> 1;
    unsigned nonce = 0;
    if (wave == 0) {
        const unsigned tags = (unsigned)(entry >> 32);
        const unsigned seen = or16(snd == j ? 0u : 1u << ((tags >> sh_j) & 15u));
        nonce = __builtin_amdgcn_readfirstlane(__builtin_ctz(~seen));
        if (lane < 16 && snd == j) {
            const unsigned nt = (tags & ~(15u << sh_j)) | (nonce << sh_j);
            __hip_atomic_store(board, ((unsigned long long)nt << 32) | (unsigned)entry, RLX_AGENT);
        }
    }

    __shared__ float red[2][16];
    __shared__ float stat[2];
    unsigned long long tf = 0;
    v2f s2 = {0.f, 0.f};
    unsigned long long cur = 0;
    #pragma unroll
    for (int k = 0; k < CPG; ++k) {
        s2 += (unpack(a[k][0]) + unpack(a[k][1])) + (unpack(a[k][2]) + unpack(a[k][3]));
        asm volatile("" : "+v"(s2) :: "memory");
        if (k == 0) { STAMP(tt); tf = tt; }
        if (k == 9 && wave == 0) cur = __hip_atomic_load(board, RLX_AGENT);
    }
    #pragma unroll
    for (int k = 0; k < CPG; ++k) asm volatile("" : "+v"(a[k]));
    STAMP(t1);
    float s = wsum(s2.x + s2.y);
    const float wm = s * (1.f / (64 * 8 * CPG));
    v2f q2v = {0.f, 0.f}, q2w = {0.f, 0.f};
    const v2f wm2 = {wm, wm};
    #pragma unroll
    for (int k = 0; k < CPG; ++k) {
        #pragma unroll
        for (int i = 0; i < 4; i += 2) {
            v2f d0 = unpack(a[k][i]) - wm2, d1 = unpack(a[k][i + 1]) - wm2;
            q2v = __builtin_elementwise_fma(d0, d0, q2v); q2w = __builtin_elementwise_fma(d1, d1, q2w);
        }
    }
    #pragma unroll
    for (int k = 0; k < CPG; ++k) asm volatile("" : "+v"(a[k]));
    float q = wsum((q2v.x + q2v.y) + (q2w.x + q2w.y));
    if (lane == 0) { red[0][wave] = wm; red[1][wave] = q; }
    __syncthreads();
    STAMP(t1b);
    if (wave == 0) {
        float m = red[0][lane & 15], qq = red[1][lane & 15];
        float ms = m;
        #pragma unroll
        for (int o = 8; o >= 1; o >>= 1) ms += __shfl_xor(ms, o);
        const float bm = ms * (1.f / 16.f);
        float d = m - bm;
        float q2 = qq + (float)(64 * 8 * CPG) * d * d;
        #pragma unroll
        for (int o = 8; o >= 1; o >>= 1) q2 += __shfl_xor(q2, o);
        const unsigned mine = __float_as_uint((lane & 1) ? q2 : bm);
        unsigned sent = 0; bool first = true;
        for (unsigned spins = 0; spins < (1u << 22); ++spins) {
            const unsigned tags = (unsigned)(cur >> 32);
            const unsigned own = (snd == j) ? (nonce << sh_j) : (tags & (15u << (4 * snd)));
            const unsigned view = __builtin_amdgcn_readfirstlane(or16((lane & 1) ? 0u : own));
            if (first || view != sent) {
                if (lane < 16 && snd == j) __hip_atomic_store(board, ((unsigned long long)view << 32) | mine, RLX_AGENT);
                sent = view; first = false;
            }
            if (__all(snd == j || ((tags >> sh_j) & 15u) == nonce)) break;
            cur = __hip_atomic_load(board, RLX_AGENT);
        }
        float v = __uint_as_float(snd == j ? mine : (unsigned)cur);
        float vm = __shfl(v, (lane & 7) * 2), vq = __shfl(v, (lane & 7) * 2 + 1);
        float gs = vm;
        #pragma unroll
        for (int o = 4; o >= 1; o >>= 1) gs += __shfl_xor(gs, o);
        const float mean = gs * (1.f / 8.f);
        float dd = vm - mean;
        float gq = vq + (float)(SEG * CPG) * dd * dd;
        #pragma unroll
        for (int o = 4; o >= 1; o >>= 1) gq += __shfl_xor(gq, o);
        const float var = gq * (1.f / (float)(CPG * HW));
        if (lane == 0) { stat[0] = mean; stat[1] = 1.0f / sqrtf(var + 1e-5f); }
    }
    __syncthreads();
    STAMP(t2);
    const float mean = stat[0], rstd = stat[1];
    const float scl = rstd * gl, shl = bl - mean * scl;
    #pragma unroll
    for (int k = 0; k < CPG; ++k) {
        const float sc = __uint_as_float(__builtin_amdgcn_readlane(__float_as_uint(scl), k));
        const float sh = __uint_as_float(__builtin_amdgcn_readlane(__float_as_uint(shl), k));
        const v2f sc2 = {sc, sc}, sh2 = {sh, sh};
        v4u o;
        #pragma unroll
        for (int i = 0; i < 4; ++i) o[i] = pack(__builtin_elementwise_fma(unpack(a[k][i]), sc2, sh2));
        __builtin_amdgcn_raw_buffer_store_b128(o, ry, t * 16 + k * HW * 2, 0, ST_AUX);
    }
    STAMP(t3);
    asm volatile("s_waitcnt vmcnt(0)" ::: "memory");
    __syncthreads();
    STAMP(t4);
    asm volatile("" :: "s"(t0), "s"(tf), "s"(t1), "s"(t1b), "s"(t2), "s"(t3), "s"(t4));
    if (wave == 0) {
        unsigned long long d = (TB - TA) * KAMP; if (d > 50000) d = 50000;
        const unsigned long long until = t4 + d;
        while (__builtin_amdgcn_s_memrealtime() < until) __builtin_amdgcn_s_sleep(2);
    }
}

extern "C" void kernel_launch(void* const* d_in, const int* in_sizes, int n_in,
                              void* d_out, int out_size, void* d_ws, size_t ws_size,
                              hipStream_t stream) {
    gn_fused<<<G * WPG, NT, 0, stream>>>(d_in[0], (const unsigned short*)d_in[1], (const unsigned short*)d_in[2],
                                          d_out, (unsigned long long*)d_ws);
}
```

```cpp
#include <hip/hip_runtime.h>
#include <hip/hip_bf16.h>
#include <stdint.h>

typedef unsigned v4u __attribute__((ext_vector_type(4)));
typedef float v2f __attribute__((ext_vector_type(2)));
typedef __bf16 v2bf __attribute__((ext_vector_type(2)));
#define GAS __attribute__((address_space(1)))
typedef GAS unsigned long long gu64;
#define RLX_AGENT __ATOMIC_RELAXED, __HIP_MEMORY_SCOPE_AGENT

constexpr int G = 32, CPG = 14, HW = 65536;
constexpr int WPG = 8;
constexpr int NT = 256;
constexpr int RPC = 1024 / NT, NL = CPG * RPC, NW = NT / 64;
constexpr int SEG = HW / WPG;

__device__ __forceinline__ v2f unpack(unsigned u) { return (v2f){__uint_as_float(u << 16), __uint_as_float(u & 0xffff0000u)}; }
__device__ __forceinline__ unsigned pack(v2f f) { v2bf r = __builtin_convertvector(f, v2bf); return __builtin_bit_cast(unsigned, r); }
__device__ __forceinline__ float wsum(float s) {
    #pragma unroll
    for (int m = 32; m >= 1; m >>= 1) s += __shfl_xor(s, m);
    return s;
}
__device__ __forceinline__ unsigned or16(unsigned v) {
    #pragma unroll
    for (int m = 8; m >= 1; m >>= 1) v |= __shfl_xor(v, m);
    return v;
}

#define STAMP(v) unsigned long long v = __builtin_amdgcn_s_memrealtime(); asm volatile("s_waitcnt lgkmcnt(0)" : "+s"(v) :: "memory")
#define KAMP 4
#define TA t0
#define TB t0
#define LD_AUX 2
#define ST_AUX 16
#define MAPPING 1
#define TLBWARM 0

__global__ void __launch_bounds__(NT, 1) gn_fused(const void* __restrict__ x, const unsigned short* __restrict__ gam,
                                                  const unsigned short* __restrict__ bet, void* __restrict__ y,
                                                  unsigned long long* __restrict__ boards) {
    STAMP(t0);
    const int b = blockIdx.x;
    const int g = (b & 7) * 4 + (b >> 6), j = (b >> 3) & 7;
    const int t = threadIdx.x, lane = t & 63;
    const int wave = __builtin_amdgcn_readfirstlane(t >> 6);
    gu64* board = (gu64*)boards + g * 32 + (lane & 15);
    unsigned long long entry = 0;
    if (wave == 0) entry = __hip_atomic_load(board, RLX_AGENT);
    __builtin_amdgcn_sched_barrier(0);

    unsigned pfw[6] = {0,0,0,0,0,0};
    { unsigned long long pc; asm volatile("s_getpc_b64 %0" : "=s"(pc));
      if (wave == 1) { for (int z = 0; z < 6; ++z) { const int ln = z * 64 + lane; pfw[z] = *(const GAS unsigned*)((pc & ~63ull) + (unsigned)(ln < 345 ? ln : 0) * 64u); } } }
    const unsigned base = (unsigned)(g * CPG * HW + j * SEG) * 2u;
    const __amdgpu_buffer_rsrc_t rx = __builtin_amdgcn_make_buffer_rsrc((char*)x + base, 0, G * CPG * HW * 2 - base, 0x00020000);
    const __amdgpu_buffer_rsrc_t ry = __builtin_amdgcn_make_buffer_rsrc((char*)y + base, 0, G * CPG * HW * 2 - base, 0x00020000);
    v4u a[NL];
    #pragma unroll
    for (int k = 0; k < NL; ++k) {
        a[k] = __builtin_amdgcn_raw_buffer_load_b128(rx, t * 16, (k / RPC) * HW * 2 + (k % RPC) * NT * 16, LD_AUX);
        __builtin_amdgcn_sched_barrier(0);
    }
    const int ck = g * CPG + (lane < CPG ? lane : 0);
    const float gl = __uint_as_float((unsigned)gam[ck] << 16), bl = __uint_as_float((unsigned)bet[ck] << 16);
    __builtin_amdgcn_sched_barrier(0);

    const int sh_j = 4 * j, snd = (lane & 15) >> 1;
    asm volatile("" :: "v"(pfw[0] ^ pfw[1] ^ pfw[2] ^ pfw[3] ^ pfw[4] ^ pfw[5]));
    unsigned nonce = 0;
    if (wave == 0) {
        const unsigned tags = (unsigned)(entry >> 32);
        const unsigned seen = or16(snd == j ? 0u : 1u << ((tags >> sh_j) & 15u));
        nonce = __builtin_amdgcn_readfirstlane(__builtin_ctz(~seen));
        if (lane < 16 && snd == j) {
            const unsigned nt = (tags & ~(15u << sh_j)) | (nonce << sh_j);
            __hip_atomic_store(board, ((unsigned long long)nt << 32) | (unsigned)entry, RLX_AGENT);
        }
    }

    __shared__ float red[2][16];
    __shared__ float pin[21000]; if (gl == 123.f && bl == 77.f) pin[t * 80] = gl; asm volatile("" :: "v"(pin));
    __shared__ float stat[2];
    unsigned long long tf = 0;
    v2f s2 = {0.f, 0.f};
    unsigned long long cur = 0;
    #pragma unroll
    for (int k = 0; k < NL; ++k) {
        s2 += (unpack(a[k][0]) + unpack(a[k][1])) + (unpack(a[k][2]) + unpack(a[k][3]));
        asm volatile("" : "+v"(s2) :: "memory");
        if (k == 0) { STAMP(tt); tf = tt; }
        if (k == NL * 2 / 3 && wave == 0) cur = __hip_atomic_load(board, RLX_AGENT);
    }
    #pragma unroll
    for (int k = 0; k < NL; ++k) asm volatile("" : "+v"(a[k]));
    STAMP(t1);
    float s = wsum(s2.x + s2.y);
    const float wm = s * (1.f / (64 * 8 * NL));
    v2f q2v = {0.f, 0.f}, q2w = {0.f, 0.f};
    const v2f wm2 = {wm, wm};
    #pragma unroll
    for (int k = 0; k < NL; ++k) {
        #pragma unroll
        for (int i = 0; i < 4; i += 2) {
            v2f d0 = unpack(a[k][i]) - wm2, d1 = unpack(a[k][i + 1]) - wm2;
            q2v = __builtin_elementwise_fma(d0, d0, q2v); q2w = __builtin_elementwise_fma(d1, d1, q2w);
        }
    }
    #pragma unroll
    for (int k = 0; k < NL; ++k) asm volatile("" : "+v"(a[k]));
    float q = wsum((q2v.x + q2v.y) + (q2w.x + q2w.y));
    if (lane == 0) { red[0][wave] = wm; red[1][wave] = q; }
    __syncthreads();
    STAMP(t1b);
    if (wave == 0) {
        float m = red[0][lane & (NW - 1)], qq = red[1][lane & (NW - 1)];
        float ms = m;
        #pragma unroll
        for (int o = NW / 2; o >= 1; o >>= 1) ms += __shfl_xor(ms, o);
        const float bm = ms * (1.f / NW);
        float d = m - bm;
        float q2 = qq + (float)(64 * 8 * NL) * d * d;
        #pragma unroll
        for (int o = NW / 2; o >= 1; o >>= 1) q2 += __shfl_xor(q2, o);
        const unsigned mine = __float_as_uint((lane & 1) ? q2 : bm);
        unsigned sent = 0; bool first = true;
        for (unsigned spins = 0; spins < (1u << 22); ++spins) {
            const unsigned tags = (unsigned)(cur >> 32);
            const unsigned own = (snd == j) ? (nonce << sh_j) : (tags & (15u << (4 * snd)));
            const unsigned view = __builtin_amdgcn_readfirstlane(or16((lane & 1) ? 0u : own));
            if (first || view != sent) {
                if (lane < 16 && snd == j) __hip_atomic_store(board, ((unsigned long long)view << 32) | mine, RLX_AGENT);
                sent = view; first = false;
            }
            if (__all(snd == j || ((tags >> sh_j) & 15u) == nonce)) break;
            cur = __hip_atomic_load(board, RLX_AGENT);
        }
        float v = __uint_as_float(snd == j ? mine : (unsigned)cur);
        float vm = __shfl(v, (lane & 7) * 2), vq = __shfl(v, (lane & 7) * 2 + 1);
        float gs = vm;
        #pragma unroll
        for (int o = 4; o >= 1; o >>= 1) gs += __shfl_xor(gs, o);
        const float mean = gs * (1.f / 8.f);
        float dd = vm - mean;
        float gq = vq + (float)(SEG * CPG) * dd * dd;
        #pragma unroll
        for (int o = 4; o >= 1; o >>= 1) gq += __shfl_xor(gq, o);
        const float var = gq * (1.f / (float)(CPG * HW));
        if (lane == 0) { stat[0] = mean; stat[1] = 1.0f / sqrtf(var + 1e-5f); }
    }
    __syncthreads();
    STAMP(t2);
    const float mean = stat[0], rstd = stat[1];
    const float scl = rstd * gl, shl = bl - mean * scl;
    #pragma unroll
    for (int k = 0; k < NL; ++k) {
        const float sc = __uint_as_float(__builtin_amdgcn_readlane(__float_as_uint(scl), k / RPC));
        const float sh = __uint_as_float(__builtin_amdgcn_readlane(__float_as_uint(shl), k / RPC));
        const v2f sc2 = {sc, sc}, sh2 = {sh, sh};
        v4u o;
        #pragma unroll
        for (int i = 0; i < 4; ++i) o[i] = pack(__builtin_elementwise_fma(unpack(a[k][i]), sc2, sh2));
        __builtin_amdgcn_raw_buffer_store_b128(o, ry, t * 16 + (k / RPC) * HW * 2 + (k % RPC) * NT * 16, 0, ST_AUX);
    }
    STAMP(t3);
    asm volatile("s_waitcnt vmcnt(0)" ::: "memory");
    __syncthreads();
    STAMP(t4);
    asm volatile("" :: "s"(t0), "s"(tf), "s"(t1), "s"(t1b), "s"(t2), "s"(t3), "s"(t4));
    if (wave == 0) {
        unsigned long long d = (TB - TA) * KAMP; if (d > 50000) d = 50000;
        const unsigned long long until = t4 + d;
        while (__builtin_amdgcn_s_memrealtime() < until) __builtin_amdgcn_s_sleep(2);
    }
}

extern "C" void kernel_launch(void* const* d_in, const int* in_sizes, int n_in,
                              void* d_out, int out_size, void* d_ws, size_t ws_size,
                              hipStream_t stream) {
    gn_fused<<<G * WPG, NT, 0, stream>>>(d_in[0], (const unsigned short*)d_in[1], (const unsigned short*)d_in[2],
                                          d_out, (unsigned long long*)d_ws);
}
```

```cpp
#include <hip/hip_runtime.h>
#include <hip/hip_bf16.h>
#include <stdint.h>

typedef unsigned v4u __attribute__((ext_vector_type(4)));
typedef float v2f __attribute__((ext_vector_type(2)));
typedef __bf16 v2bf __attribute__((ext_vector_type(2)));
typedef unsigned long long u64;
#define GAS __attribute__((address_space(1)))
#define LAS __attribute__((address_space(3)))
typedef GAS u64 gu64;
#define RLX_AGENT __ATOMIC_RELAXED, __HIP_MEMORY_SCOPE_AGENT

constexpr int CPG = 14, HW = 65536, NGRP = CPG * HW;
constexpr int PPG = NGRP / 512;
constexpr int PPU = 8, NP = 4 * PPU;
constexpr int NUNIT = PPU * 512;
constexpr int NDW = 7, NT = (NDW + 1) * 64;
constexpr int AUX_NT = 2, AUX_SC1 = 16;
constexpr unsigned SPIN_CAP = 1u << 20;
constexpr int ld_due(int k) { return (188 * k + (k >= 3 * PPU ? 16 * (k - (3 * PPU - 1)) : 0)) / 8; }
constexpr int st_due(int k) { return 787 + 184 * k / 8; }
constexpr int XCC0_LEAD = 16;
constexpr int N0 = 3;
constexpr int CAP = 6;
constexpr int K_VIEW = 11, K_PUB0 = 15, K_PUB1 = 2 * PPU - 1 + CAP, K_HAND0 = 30;
constexpr int J_PUB3 = 4, J_HAND2 = 2 * PPU, J_HAND3 = 3 * PPU;
constexpr unsigned CODE_BLOCKS = 197;

#define BARRIER() asm volatile("s_waitcnt lgkmcnt(0)\n\ts_barrier" ::: "memory")
#define WAITV(n) asm volatile("s_waitcnt vmcnt(%0)" :: "n"(n) : "memory")
#define FENCE() __builtin_amdgcn_sched_barrier(0)

__device__ __forceinline__ v2f unpack(unsigned u, unsigned himask) { return (v2f){__uint_as_float(u << 16), __uint_as_float(u & himask)}; }
__device__ __forceinline__ unsigned pack(v2f f) { return __builtin_bit_cast(unsigned, __builtin_convertvector(f, v2bf)); }
__device__ __forceinline__ float bf16_at(const void* p, int i) { return __uint_as_float((unsigned)((const GAS unsigned short*)p)[i] << 16); }
__device__ __forceinline__ u64 mk64(unsigned lo, unsigned hi) { return ((u64)hi << 32) | lo; }
__device__ __forceinline__ u64 ballot(bool p) { return __builtin_amdgcn_ballot_w64(p); }
__device__ __forceinline__ float bcast(float v, int l) { return __uint_as_float(__builtin_amdgcn_readlane(__float_as_uint(v), l)); }
template <int CTRL, int ROWMASK> __device__ __forceinline__ float dpp_add(float v) {
    return v + __uint_as_float(__builtin_amdgcn_update_dpp(0u, __float_as_uint(v), CTRL, ROWMASK, 0xf, true));
}
__device__ __forceinline__ float row_sum(float v) {
    v = dpp_add<0x111, 0xf>(v); v = dpp_add<0x112, 0xf>(v); v = dpp_add<0x114, 0xf>(v); return dpp_add<0x118, 0xf>(v);
}
__device__ __forceinline__ float half_sum(float v) { return dpp_add<0x142, 0xa>(row_sum(v)); }
__device__ __forceinline__ float wave_sum63(float v) { return dpp_add<0x143, 0xc>(half_sum(v)); }

template <bool STORE, int K = 0> __device__ __forceinline__ void pace(int k, unsigned t0) {
    if constexpr (K < NP) {
        if (k != K) return pace<STORE, K + 1>(k, t0);
        asm volatile("1:\n\ts_memrealtime vcc\n\ts_waitcnt lgkmcnt(0)\n\ts_sub_i32 vcc_lo, vcc_lo, %0\n\t"
                     "s_cmpk_lt_i32 vcc_lo, %1\n\ts_cbranch_scc1 1b"
                     :: "s"(t0), "n"(STORE ? st_due(K) : ld_due(K)) : "memory", "scc", "vcc");
    }
}

__device__ __forceinline__ void fetch_code(int wave) {
    const unsigned o = min((blockIdx.x >> 3) * 7u, CODE_BLOCKS - 7u) * 64u;
    unsigned d0, d1, d2, d3, d4, d5, d6; u64 pc;
    asm volatile("s_cmp_lg_u32 %9, %10\n\ts_cbranch_scc1 1f\n\ts_getpc_b64 %7\n\t"
                 "s_load_dword %0, %7, %8 offset:0\n\ts_load_dword %1, %7, %8 offset:64\n\ts_load_dword %2, %7, %8 offset:128\n\t"
                 "s_load_dword %3, %7, %8 offset:192\n\ts_load_dword %4, %7, %8 offset:256\n\ts_load_dword %5, %7, %8 offset:320\n\t"
                 "s_load_dword %6, %7, %8 offset:384\n\ts_waitcnt lgkmcnt(0)\n1:"
                 : "=&s"(d0), "=&s"(d1), "=&s"(d2), "=&s"(d3), "=&s"(d4), "=&s"(d5), "=&s"(d6), "=&s"(pc)
                 : "s"(o), "s"(wave), "n"(NDW) : "scc");
}

struct Acc { float s0, s1, q[4], piv; };
__device__ __forceinline__ float dot2(unsigned a, unsigned b, float c) {
    return __builtin_amdgcn_fdot2_f32_bf16(__builtin_bit_cast(v2bf, a), __builtin_bit_cast(v2bf, b), c, false);
}
__device__ __forceinline__ void acc_piece(Acc& A, const v4u& a, bool first, unsigned ones) {
    if (first) {
        A.piv = __uint_as_float(__builtin_amdgcn_readfirstlane(a[0] << 16));
        A.s0 = A.s1 = A.q[0] = A.q[1] = A.q[2] = A.q[3] = 0.f;
    }
    #pragma unroll
    for (int i = 0; i < 4; i += 2) {
        A.s0 = dot2(ones, a[i], A.s0);         A.q[i] = dot2(a[i], a[i], A.q[i]);
        A.s1 = dot2(ones, a[i + 1], A.s1);     A.q[i + 1] = dot2(a[i + 1], a[i + 1], A.q[i + 1]);
    }
    asm volatile("" : "+v"(A.s0), "+v"(A.s1), "+v"(A.q[0]), "+v"(A.q[1]), "+v"(A.q[2]), "+v"(A.q[3]));
}
__device__ __forceinline__ void acc_finish(const Acc& A, LAS v2f* slot, int lane) {
    const float K = A.piv, nK = -(float)(4 * PPU) * K;
    const float d = fmaf(nK, K, A.q[0] + A.q[1]) + fmaf(nK, K, A.q[2] + A.q[3]);
    const float sl = fmaf(2.f, nK, A.s0 + A.s1);
    const float S = wave_sum63(sl);
    const float Q = wave_sum63(fmaf(-2.f * K, sl, d));
    const float dm = S * (1.f / NUNIT);
    if (lane == 63) *slot = (v2f){K + dm, fmaf(-S, dm, Q)};
}

__device__ __forceinline__ int group_of(int q, int team) {
    return team + (((team < 3 ? 0x10080013u : 0x18080013u) >> (8 * q)) & 0xff);
}

__global__ void __launch_bounds__(NT, 2) gn_fused(const void* __restrict__ x, const void* __restrict__ gam,
                                                  const void* __restrict__ bet, void* __restrict__ y,
                                                  u64* __restrict__ ws) {
    __shared__ v2f lds[2 * 32];
    LAS v2f* const L = (LAS v2f*)lds;
    const int team = blockIdx.x >> 5, me = blockIdx.x & 31;
    const int lane = threadIdx.x & 63;
    const int wave = __builtin_amdgcn_readfirstlane(threadIdx.x >> 6);
    fetch_code(wave);
    const int unit = me * NDW + wave;
    const unsigned base = (unsigned)(team * PPG + unit * PPU) * 1024u;
    constexpr unsigned SPAN = (24 * PPG + PPU) * 1024u;
    const unsigned rot = (unit & 2) << 11;
    auto soff = [&](int s) { return (group_of(s / PPU, team) - team) * (PPG * 1024u) + (((s & 4) * 1024u) ^ rot); };
    const __amdgpu_buffer_rsrc_t rx = __builtin_amdgcn_make_buffer_rsrc((char*)x + base, 0, SPAN, 0x00020000);
    const unsigned voff = lane * 16;
    asm volatile("" :: "s"(rx), "s"(rot), "s"(gam), "s"(bet), "s"(y), "s"(ws), "v"(voff));

    if (wave == NDW) {
        __builtin_amdgcn_s_setprio(3);
        gu64* const board = (gu64*)ws + team * 320 + (lane & 31) * 2 + (lane >> 5);
        gu64* const reqs = board + 256;
        const bool own = (lane & 31) == me;
        const u64 others = ~(0x100000001ull << me);
        unsigned e[4];
        #pragma unroll
        for (int q = 0; q < 4; ++q) e[q] = __hip_atomic_load((GAS unsigned*)(board + 64 * q) + 1, RLX_AGENT);
        const int ch = group_of((lane >> 3) & 3, team) * CPG + ((me * NDW + min(lane & 7, NDW - 1)) >> 4);
        const float ga = bf16_at(gam, ch), be = bf16_at(bet, ch);
        asm volatile("" : "+v"(e[0]), "+v"(e[1]), "+v"(e[2]), "+v"(e[3]) :: "memory");
        const u64 my0 = ~ballot((e[0] >> me) & 1), my1 = ~ballot((e[1] >> me) & 1);
        if (own) __hip_atomic_store(reqs, mk64((unsigned)(my0 >> (lane & 32)), (unsigned)(my1 >> (lane & 32))), RLX_AGENT);

        auto partial = [&](int q) {
            const v2f w = L[8 * q + (lane & 7)];
            const bool in = (lane & 15) < NDW;
            const float m = bcast(row_sum(in ? w.x : 0.f), 15) * (1.f / NDW), d = w.x - m;
            const float m2 = bcast(row_sum(in ? fmaf(d * (float)NUNIT, d, w.y) : 0.f), 15);
            return __float_as_uint(lane >= 32 ? m2 : m);
        };
        auto finalize = [&](int q, unsigned mine, u64 r) {
            const float v = __uint_as_float(own ? mine : (unsigned)r);
            const float hs = half_sum(v);
            const float mean = bcast(hs, 31) * (1.f / 32);
            const float dd = v - mean;
            const float m2 = fmaf(bcast(half_sum(dd * dd), 31), (float)(NDW * NUNIT), bcast(hs, 63));
            const float sc = __builtin_amdgcn_rsqf(fmaf(fmaxf(m2, 0.f), 1.f / (float)NGRP, 1e-5f)) * ga;
            if ((lane >> 3) == q) L[32 + lane] = (v2f){sc, fmaf(-mean, sc, be)};
        };
        auto send = [&](int q, unsigned val, unsigned tag) {
            if (own) __hip_atomic_store(board + 64 * q, mk64(val, tag), RLX_AGENT);
        };
        auto half = [&](u64 bits) { return (unsigned)(bits >> (lane & 32)); };
        auto wanted = [&](unsigned rq) { return ballot((rq >> me) & 1) & others; };
        auto fresh = [&](u64 r, u64 my) { return ((ballot(((unsigned)(r >> 32) >> me) & 1) ^ my) & others) == 0; };
        auto collect = [&](int q) {
            for (unsigned spins = 0;; ++spins) {
                const u64 r = __hip_atomic_load(board + 64 * q, RLX_AGENT);
                if ((ballot((unsigned)(r >> 32) == e[q]) & others) == 0 || spins >= SPIN_CAP) return r;
                __builtin_amdgcn_s_sleep(16);
            }
        };

        BARRIER();
        u64 rq = __hip_atomic_load(reqs, RLX_AGENT);
        BARRIER();
        const unsigned v0 = partial(0);
        WAITV(0);
        u64 sent0 = wanted((unsigned)rq), sent1 = wanted((unsigned)(rq >> 32));
        send(0, v0, half(sent0));
        BARRIER();
        const unsigned v1 = partial(1);
        send(1, v1, half(sent1));
        auto answer = [&]() {
            const u64 w0 = wanted((unsigned)rq), w1 = wanted((unsigned)(rq >> 32));
            if (w0 != sent0 || w1 != sent1) { sent0 = w0; sent1 = w1; send(0, v0, half(w0)); send(1, v1, half(w1)); }
        };
        u64 r0;
        for (unsigned spins = 0;; ++spins) {
            rq = __hip_atomic_load(reqs, RLX_AGENT);
            r0 = __hip_atomic_load(board, RLX_AGENT);
            answer();
            if (fresh(r0, my0) || spins >= SPIN_CAP) break;
            __builtin_amdgcn_s_sleep(2);
        }
        finalize(0, v0, r0);
        FENCE();
        WAITV(0);
        rq = __hip_atomic_load(reqs, RLX_AGENT);
        u64 r1 = __hip_atomic_load(board + 64, RLX_AGENT);
        BARRIER();
        const unsigned v2 = partial(2);
        send(2, v2, e[2] + 1);
        BARRIER();
        FENCE();
        answer();
        for (unsigned spins = 0; !fresh(r1, my1) && spins < SPIN_CAP; ++spins) {
            __builtin_amdgcn_s_sleep(2);
            r1 = __hip_atomic_load(board + 64, RLX_AGENT);
        }
        finalize(1, v1, r1);
        BARRIER();
        const unsigned v3 = partial(3);
        send(3, v3, e[3] + 1);
        finalize(2, v2, collect(2));
        BARRIER();
        finalize(3, v3, collect(3));
        BARRIER();
        return;
    }

    unsigned hm = 0xffff0000u, ones = 0x3f803f80u; asm volatile("" : "+s"(hm), "+s"(ones));
    unsigned t0 = 0;
    v4u a[NP];
    Acc acc;
    v2f sc2, sh2;
    auto stats = [&](int k) {
        acc_piece(acc, a[k], k % PPU == 0, ones);
        if (k % PPU == PPU - 1) acc_finish(acc, L + (k / PPU) * 8 + wave, lane);
        FENCE();
    };
    auto affine = [&](int q) { const v2f st = L[32 + 8 * q + wave]; sc2 = (v2f){st.x, st.x}; sh2 = (v2f){st.y, st.y}; };
    auto normalize = [&](int j) {
        #pragma unroll
        for (int i = 0; i < 4; ++i) a[j][i] = pack(__builtin_elementwise_fma(unpack(a[j][i], hm), sc2, sh2));
        asm volatile("" : "+v"(a[j]));
        FENCE();
    };
    static_assert(K_VIEW < K_PUB0 && PPU - 1 + CAP <= K_PUB0 && K_PUB0 < K_PUB1 && 3 * PPU - 1 + CAP <= K_HAND0 && K_HAND0 < NP, "");
    static_assert(J_PUB3 <= PPU, "");
    #pragma unroll
    for (int k = 0; k < NP; ++k) {
        if (k >= CAP) WAITV(CAP - 1);
        if (k >= N0) pace<false>(k, t0);
        a[k] = __builtin_amdgcn_raw_buffer_load_b128(rx, voff + (k & 3) * 1024, soff(k), AUX_NT);
        FENCE();
        if (k == N0 - 1) {
            const bool xcc0 = __builtin_amdgcn_s_getreg(20 | (3 << 11)) == 0;
            t0 = (unsigned)__builtin_amdgcn_s_memrealtime() - (xcc0 ? XCC0_LEAD : 0);
            FENCE();
        }
        if (k >= CAP) stats(k - CAP);
        if (k == K_VIEW || k == K_PUB0 || k == K_PUB1 || k == K_HAND0) BARRIER();
        if (k == K_HAND0) { affine(0); normalize(0); normalize(1); }
    }
    #pragma unroll
    for (int k = NP - CAP; k < NP - 1; ++k) stats(k);
    BARRIER();
    const __amdgpu_buffer_rsrc_t ry = __builtin_amdgcn_make_buffer_rsrc((char*)y + base, 0, SPAN, 0x00020000);
    #pragma unroll
    for (int j = 0; j < NP; ++j) {
        if (j == J_PUB3) stats(NP - 1);
        if (j == J_PUB3 || j == J_HAND2 || j == J_HAND3) BARRIER();
        if (j > 0 && j % PPU == 0) affine(j / PPU);
        if (j >= 2) normalize(j);
        pace<true>(j, t0);
        __builtin_amdgcn_raw_buffer_store_b128(a[j], ry, voff + (j & 3) * 1024, soff(j), AUX_SC1);
        asm volatile("s_nop 1" :: "v"(a[j]) : "memory");
        FENCE();
    }
}

extern "C" void kernel_launch(void* const* d_in, const int* in_sizes, int n_in,
                              void* d_out, int out_size, void* d_ws, size_t ws_size,
                              hipStream_t stream) {
    gn_fused<<<256, NT, 0, stream>>>(d_in[0], d_in[1], d_in[2], d_out, (u64*)d_ws);
}
```
